```python
import torch

def _softmax(x, axis=-1):
    return torch.softmax(x, dim=axis)
def _gelu(x):
    return torch.nn.functional.gelu(x, approximate="tanh")
def _silu(x):
    return torch.nn.functional.silu(x)
def _sigmoid(x):
    return torch.sigmoid(x)
def _layernorm(x, axis=-1, eps=1e-5):
    m = x.mean(dim=axis, keepdim=True)
    v = x.var(dim=axis, keepdim=True, unbiased=False)
    return (x - m) / torch.sqrt(v + eps)
def _scatter_add(tgt, idx, src, axis=0):
    return tgt.clone().index_add_(axis, idx, src)
def _segment_sum(src, seg, n):
    out = torch.zeros((n,) + src.shape[1:], dtype=src.dtype)
    return out.index_add_(0, seg, src)
def _pad(x, pw):
    flat = tuple(v for pair in reversed(pw) for v in pair)
    return torch.nn.functional.pad(x, flat)

kernel_name = 'gated_mix__mha_block__ssm_conv_s4d__prompt'

def setup_inputs():

    _w = (lambda a: (lambda t: t.to(torch.bfloat16) if t.is_floating_point() else t)(torch.as_tensor(a)))
    def _A_setup():
        B, S, D, H = 1, 2048, 576, 32
        return {
            "x": _w((lambda *s: torch.randn(*s, dtype=torch.bfloat16) * 0.05)(B, S, D)),
            "wqkv": _w((lambda *s: torch.randn(*s, dtype=torch.bfloat16) * 0.8)(D, 3 * D)),
            "wo": _w((lambda *s: torch.randn(*s, dtype=torch.bfloat16) * 0.05)(D, D)),
        }
    _A_in = _A_setup()

    def _B_setup():
        Bsz, L, D, N = 1, 2048, 576, 128
        return {
            "u": _w((lambda *s: torch.randn(*s, dtype=torch.bfloat16) * 0.05)(Bsz, L, D)),
            "log_A_real": _w((lambda *s: torch.randn(*s, dtype=torch.bfloat16) * 6.4)(D, N) * 0.5),
            "A_imag": _w((lambda *s: torch.randn(*s, dtype=torch.bfloat16) * 0.1)(D, N) * 3.14),
            "C_real": _w((lambda *s: torch.randn(*s, dtype=torch.bfloat16) * 0.4)(D, N) / N ** 0.5),
            "C_imag": _w((lambda *s: torch.randn(*s, dtype=torch.bfloat16) * 0.4)(D, N) / N ** 0.5),
            "log_dt": _w((lambda *s: torch.randn(*s, dtype=torch.bfloat16) * 0.1)(D) * 0.5 - 4.0),
            "D_skip": _w((lambda *s: torch.randn(*s, dtype=torch.bfloat16) * 0.1)(D)),
        }
    _B_in = _B_setup()

    return {
        "A_x": _A_in["x"],
        "A_wqkv": _A_in["wqkv"],
        "A_wo": _A_in["wo"],
        "B_u": _B_in["u"],
        "B_log_A_real": _B_in["log_A_real"],
        "B_A_imag": _B_in["A_imag"],
        "B_C_real": _B_in["C_real"],
        "B_C_imag": _B_in["C_imag"],
        "B_log_dt": _B_in["log_dt"],
        "B_D_skip": _B_in["D_skip"],
        "gate_w": _w((lambda *s: torch.randn(*s, dtype=torch.bfloat16) * 0.05)(576) * 16.0),
    }

def reference(A_x, A_wqkv, A_wo, B_u, B_log_A_real, B_A_imag, B_C_real, B_C_imag, B_log_dt, B_D_skip, gate_w):
    def _A_ref(x, wqkv, wo):
        B, S, D = x.shape
        H = 32
        Dh = D // H
        qkv = (lambda x, *d: x.permute(*d))((x @ wqkv).reshape(B, S, 3, H, Dh), 2, 0, 3, 1, 4)
        q, k, v = qkv[0], qkv[1], qkv[2]
        scores = torch.einsum("bhqd,bhkd->bhqk", q, k) * (Dh ** -0.5)
        p = _softmax(scores, axis=-1)
        o = torch.einsum("bhqk,bhkd->bhqd", p, v)
        return (lambda x, *d: x.permute(*d))(o, 0, 2, 1, 3).reshape(B, S, D) @ wo

    def _B_ref(u, log_A_real, A_imag, C_real, C_imag, log_dt, D_skip):
        L = u.shape[1]
        dt = torch.exp(log_dt)
        A = -torch.exp(log_A_real) + 1j * A_imag
        C = (C_real + 1j * C_imag) * dt[:, None]
        pos = (lambda x, dt: x.to(dt))(torch.arange(L), log_dt.dtype)
        P = torch.exp((A * dt[:, None])[:, :, None] * pos[None, None, :])
        K = 2.0 * torch.einsum("dn,dnl->dl", C, P).real
        L2 = 2 * L
        u_t = (lambda x, *d: x.permute(*d))(u, 0, 2, 1)
        Kf = (lambda x, *a, **k: torch.fft.rfft(x.float(), *a, **k))(K, L2)
        Uf = (lambda x, *a, **k: torch.fft.rfft(x.float(), *a, **k))(u_t, L2)
        y = (lambda x, *a, **k: torch.fft.irfft(x, *a, **k).to(torch.bfloat16))(Uf * Kf[None, :, :], L2)[:, :, :L]
        y = (lambda x, *d: x.permute(*d))(y, 0, 2, 1)
        return y + u * D_skip[None, None, :]

    x = A_x
    g = _sigmoid((x * gate_w).sum(-1, keepdims=True))
    a = _A_ref(x, A_wqkv, A_wo)
    b = _B_ref(x, B_log_A_real, B_A_imag, B_C_real, B_C_imag, B_log_dt, B_D_skip)
    return g * a + (1 - g) * b
```

```cpp
#include <hip/hip_runtime.h>
#include <hip/hip_bf16.h>
#include <stdint.h>

typedef __attribute__((ext_vector_type(8))) short bf16x8;
typedef __attribute__((ext_vector_type(4))) float f32x4;
typedef unsigned short u16;

constexpr int SEQ = 2048, DM = 576, NH = 32, DH = 18, NST = 128;

__device__ __forceinline__ float bf2f(u16 v) { return __uint_as_float(((unsigned)v) << 16); }
__device__ __forceinline__ u16 f2bf(float f) {
  unsigned u = __float_as_uint(f);
  u += 0x7FFFu + ((u >> 16) & 1u);
  return (u16)(u >> 16);
}
__device__ __forceinline__ float rbf(float f) { return bf2f(f2bf(f)); }
__device__ __forceinline__ unsigned pack2(u16 a, u16 b) { return (unsigned)a | ((unsigned)b << 16); }

constexpr size_t WS_QP = 0;
constexpr size_t WS_KP = WS_QP + (size_t)NH * SEQ * 32 * 2;
constexpr size_t WS_VT = WS_KP + (size_t)NH * SEQ * 32 * 2;
constexpr size_t WS_O = WS_VT + (size_t)NH * SEQ * 32 * 2;
constexpr size_t WS_BT = WS_O + (size_t)SEQ * DM * 2;

constexpr int SSM_BLOCKS = DM;
constexpr int GEMM1_BLOCKS = (SEQ / 128) * 12;
constexpr int CS = 2096;

constexpr int L_PRM = 0;
constexpr int L_KC = 2048;
constexpr int L_U = 4640;
constexpr int L_TZ = 9760;
constexpr int L_SSM_END = L_TZ + 16 * CS * 2;
constexpr int SMEM1 = L_SSM_END;

__device__ __forceinline__ float kc_lookup(const float* Kc, int l) {
  if (l < 0) return 0.f;
  int t = (int)rbf((float)l);
  int idx;
  if (t <= 256) idx = t;
  else if (t <= 512) idx = 256 + ((t - 256) >> 1);
  else if (t <= 1024) idx = 384 + ((t - 512) >> 2);
  else idx = 512 + ((t - 1024) >> 3);
  return Kc[idx];
}

__device__ void ssm_block(int d, const u16* __restrict__ x, const u16* __restrict__ lar,
                          const u16* __restrict__ aim, const u16* __restrict__ cre,
                          const u16* __restrict__ cim, const u16* __restrict__ ldt,
                          const u16* __restrict__ dsk, u16* __restrict__ BT, char* smem) {
  const int tid = threadIdx.x;
  const int lane = tid & 63, wv = tid >> 6;
  float4* prm = (float4*)(smem + L_PRM);
  float* Kc = (float*)(smem + L_KC);
  u16* Ul = (u16*)(smem + L_U);
  u16* Tz = (u16*)(smem + L_TZ);

  u16 uv[8];
#pragma unroll
  for (int i = 0; i < 8; ++i) uv[i] = x[(size_t)(tid + 256 * i) * DM + d];

  const float dt = rbf(expf(bf2f(ldt[d])));
  if (tid < NST) {
    float ar = -rbf(expf(bf2f(lar[d * NST + tid]))) * dt;
    float ai = bf2f(aim[d * NST + tid]) * dt;
    float cr = bf2f(cre[d * NST + tid]) * dt;
    float ci = bf2f(cim[d * NST + tid]) * dt;
    prm[tid] = make_float4(ar * 1.44269504088896f, ai * 0.15915494309189535f, 2.f * cr, 2.f * ci);
  }
  for (int i = tid; i < 16 * 32; i += 256) Ul[i] = 0;
#pragma unroll
  for (int i = 0; i < 8; ++i) Ul[16 * 32 + tid + 256 * i] = uv[i];
  __syncthreads();

  for (int idx = tid; idx < 641; idx += 256) {
    int t;
    if (idx <= 256) t = idx;
    else if (idx <= 384) t = 256 + 2 * (idx - 256);
    else if (idx <= 512) t = 512 + 4 * (idx - 384);
    else t = 1024 + 8 * (idx - 512);
    float tf = (float)t;
    float acc = 0.f;
#pragma unroll 4
    for (int n = 0; n < NST; ++n) {
      float4 p = prm[n];
      float e = __builtin_amdgcn_exp2f(p.x * tf);
      float ph = p.y * tf;
      float s = __builtin_amdgcn_sinf(ph);
      float c = __builtin_amdgcn_cosf(ph);
      acc += e * (p.z * c - p.w * s);
    }
    Kc[idx] = acc;
  }
  __syncthreads();

  for (int pb = tid; pb < CS / 8; pb += 256) {
    int q0 = pb * 8;
    u16 hi[15], lo[15];
#pragma unroll
    for (int i = 0; i < 15; ++i) {
      float k = kc_lookup(Kc, 2047 - (q0 + i));
      u16 h = f2bf(k);
      hi[i] = h;
      lo[i] = f2bf(k - bf2f(h));
    }
#pragma unroll
    for (int m = 0; m < 8; ++m) {
      uint4 vh = make_uint4(pack2(hi[m], hi[m + 1]), pack2(hi[m + 2], hi[m + 3]),
                            pack2(hi[m + 4], hi[m + 5]), pack2(hi[m + 6], hi[m + 7 < 15 ? m + 7 : 14]));
      uint4 vl = make_uint4(pack2(lo[m], lo[m + 1]), pack2(lo[m + 2], lo[m + 3]),
                            pack2(lo[m + 4], lo[m + 5]), pack2(lo[m + 6], lo[m + 7 < 15 ? m + 7 : 14]));
      *(uint4*)(Tz + m * CS + q0) = vh;
      *(uint4*)(Tz + (8 + m) * CS + q0) = vl;
    }
  }
  __syncthreads();

  f32x4 acc[2][4];
#pragma unroll
  for (int a = 0; a < 2; ++a)
#pragma unroll
    for (int b = 0; b < 4; ++b) acc[a][b] = (f32x4){0.f, 0.f, 0.f, 0.f};
  const int col = lane & 15, g = lane >> 4;
  for (int dl = wv; dl < 64; dl += 4) {
    bf16x8 ah[2], al[2];
#pragma unroll
    for (int rt = 0; rt < 2; ++rt) {
      int r = 16 * rt + col;
      int o = 2047 - 32 * dl - r + 8 * g;
      int m = o & 7;
      ah[rt] = *(const bf16x8*)(Tz + m * CS + (o - m));
      al[rt] = *(const bf16x8*)(Tz + (8 + m) * CS + (o - m));
    }
    int nt0 = dl >> 4;
#pragma unroll
    for (int nt = 0; nt < 4; ++nt) {
      if (nt >= nt0) {
        int ch = 16 * nt + col - dl + 16;
        bf16x8 b = *(const bf16x8*)(Ul + ch * 32 + 8 * g);
#pragma unroll
        for (int rt = 0; rt < 2; ++rt) {
          acc[rt][nt] = __builtin_amdgcn_mfma_f32_16x16x32_bf16(ah[rt], b, acc[rt][nt], 0, 0, 0);
          acc[rt][nt] = __builtin_amdgcn_mfma_f32_16x16x32_bf16(al[rt], b, acc[rt][nt], 0, 0, 0);
        }
      }
    }
  }
  __syncthreads();
  float* part = (float*)(smem + L_TZ);
#pragma unroll
  for (int rt = 0; rt < 2; ++rt)
#pragma unroll
    for (int nt = 0; nt < 4; ++nt) {
      int l0 = 32 * (16 * nt + col) + 16 * rt + 4 * g;
      *(f32x4*)(part + wv * 2048 + l0) = acc[rt][nt];
    }
  __syncthreads();
  const float dk = bf2f(dsk[d]);
#pragma unroll
  for (int i = 0; i < 8; ++i) {
    int l = tid + 256 * i;
    float y = part[l] + part[2048 + l] + part[4096 + l] + part[6144 + l];
    float r = rbf(y) + rbf(bf2f(uv[i]) * dk);
    BT[(size_t)d * SEQ + l] = f2bf(r);
  }
}

constexpr int G1_LDA = 72;
__device__ void qkv_block(int bid, const u16* __restrict__ x, const u16* __restrict__ w,
                          u16* __restrict__ Qp, u16* __restrict__ Kp, u16* __restrict__ Vt, char* smem) {
  const int tid = threadIdx.x;
  const int lane = tid & 63, wv = tid >> 6;
  const int col = lane & 15, g = lane >> 4;
  const int mt = bid / 12, ntile = bid % 12;
  const int m0 = mt * 128, n0 = ntile * 144;
  u16* sA = (u16*)smem;
  u16* sB = (u16*)(smem + 128 * G1_LDA * 2);

  f32x4 acc[2][9];
#pragma unroll
  for (int a = 0; a < 2; ++a)
#pragma unroll
    for (int b = 0; b < 9; ++b) acc[a][b] = (f32x4){0.f, 0.f, 0.f, 0.f};

  for (int k0 = 0; k0 < DM; k0 += 64) {
#pragma unroll
    for (int i = 0; i < 4; ++i) {
      int c = tid + 256 * i;
      int r = c >> 3, kc = c & 7;
      uint4 v = *(const uint4*)(x + (size_t)(m0 + r) * DM + k0 + kc * 8);
      *(uint4*)(sA + r * G1_LDA + kc * 8) = v;
    }
    for (int it = tid; it < 576; it += 256) {
      int kp = it / 18, ch = it % 18;
      const u16* src = w + (size_t)(k0 + 2 * kp) * (3 * DM) + n0 + ch * 8;
      uint4 v0 = *(const uint4*)(src);
      uint4 v1 = *(const uint4*)(src + 3 * DM);
      const u16* a0 = (const u16*)&v0;
      const u16* a1 = (const u16*)&v1;
#pragma unroll
      for (int i = 0; i < 8; ++i)
        *(unsigned*)(sB + (ch * 8 + i) * G1_LDA + 2 * kp) = pack2(a0[i], a1[i]);
    }
    __syncthreads();
#pragma unroll
    for (int kk = 0; kk < 2; ++kk) {
      bf16x8 af[2];
#pragma unroll
      for (int mi = 0; mi < 2; ++mi)
        af[mi] = *(const bf16x8*)(sA + (32 * wv + 16 * mi + col) * G1_LDA + kk * 32 + 8 * g);
#pragma unroll
      for (int ni = 0; ni < 9; ++ni) {
        bf16x8 bfr = *(const bf16x8*)(sB + (16 * ni + col) * G1_LDA + kk * 32 + 8 * g);
#pragma unroll
        for (int mi = 0; mi < 2; ++mi)
          acc[mi][ni] = __builtin_amdgcn_mfma_f32_16x16x32_bf16(af[mi], bfr, acc[mi][ni], 0, 0, 0);
      }
    }
    __syncthreads();
  }
  constexpr int LDC = 152;
  u16* sC = (u16*)smem;
#pragma unroll
  for (int mi = 0; mi < 2; ++mi)
#pragma unroll
    for (int ni = 0; ni < 9; ++ni)
#pragma unroll
      for (int r = 0; r < 4; ++r)
        sC[(32 * wv + 16 * mi + 4 * g + r) * LDC + 16 * ni + col] = f2bf(acc[mi][ni][r]);
  __syncthreads();
  const int typ = ntile >> 2;
  const int h0 = (ntile & 3) * 8;
  if (typ < 2) {
    u16* dst = typ == 0 ? Qp : Kp;
    for (int it = tid; it < 4096; it += 256) {
      int c = it & 3, hh = (it >> 2) & 7, r = it >> 5;
      const u16* s = sC + r * LDC + hh * 18 + c * 8;
      uint4 v = make_uint4(0, 0, 0, 0);
      if (c < 2) {
        v.x = *(const unsigned*)(s);
        v.y = *(const unsigned*)(s + 2);
        v.z = *(const unsigned*)(s + 4);
        v.w = *(const unsigned*)(s + 6);
      } else if (c == 2) {
        v.x = *(const unsigned*)(s);
      }
      *(uint4*)(dst + ((size_t)(h0 + hh) * SEQ + m0 + r) * 32 + c * 8) = v;
    }
  } else {
    for (int it = tid; it < 4096; it += 256) {
      int gg = it & 3, dh = (it >> 2) & 31, kt = (it >> 7) & 3, hh = it >> 9;
      u16 e[8];
#pragma unroll
      for (int j = 0; j < 8; ++j) {
        int key = (j < 4) ? (4 * gg + j) : (16 + 4 * gg + j - 4);
        u16 val;
        if (dh < 18) val = sC[(kt * 32 + key) * LDC + hh * 18 + dh];
        else val = (dh == 18) ? (u16)0x3F80 : (u16)0;
        e[j] = val;
      }
      uint4 v = make_uint4(pack2(e[0], e[1]), pack2(e[2], e[3]), pack2(e[4], e[5]), pack2(e[6], e[7]));
      size_t kt_g = (size_t)(m0 / 32 + kt);
      *(uint4*)(Vt + (((size_t)(h0 + hh) * 64 + kt_g) * 32 + dh) * 32 + gg * 8) = v;
    }
  }
}

__global__ __launch_bounds__(256) void k1_kernel(const u16* __restrict__ x, const u16* __restrict__ wqkv,
                                                 const u16* __restrict__ lar, const u16* __restrict__ aim,
                                                 const u16* __restrict__ cre, const u16* __restrict__ cim,
                                                 const u16* __restrict__ ldt, const u16* __restrict__ dsk,
                                                 u16* __restrict__ Qp, u16* __restrict__ Kp,
                                                 u16* __restrict__ Vt, u16* __restrict__ BT) {
  extern __shared__ __attribute__((aligned(16))) char smem[];
  int bid = blockIdx.x;
  if (bid < SSM_BLOCKS) ssm_block(bid, x, lar, aim, cre, cim, ldt, dsk, BT, smem);
  else qkv_block(bid - SSM_BLOCKS, x, wqkv, Qp, Kp, Vt, smem);
}

__global__ __launch_bounds__(256) void attn_kernel(const u16* __restrict__ Qp, const u16* __restrict__ Kp,
                                                   const u16* __restrict__ Vt, u16* __restrict__ O) {
  __shared__ __attribute__((aligned(16))) u16 sK[2][64 * 32];
  __shared__ __attribute__((aligned(16))) u16 sV[2][64 * 32];
  const int tid = threadIdx.x;
  const int lane = tid & 63, wv = tid >> 6;
  const int col = lane & 15, g = lane >> 4;
  const int bid = blockIdx.x;
  const int h = (bid & 7) + 8 * ((bid >> 3) & 3);
  const int qt = bid >> 5;
  const int q0 = qt * 128 + wv * 32;
  const u16* Kh = Kp + (size_t)h * SEQ * 32;
  const u16* Vh = Vt + (size_t)h * SEQ * 32;

  bf16x8 qf[2];
#pragma unroll
  for (int s = 0; s < 2; ++s)
    qf[s] = *(const bf16x8*)(Qp + ((size_t)h * SEQ + q0 + 16 * s + col) * 32 + 8 * g);

  f32x4 oacc[2][2];
  float mrun[2];
#pragma unroll
  for (int s = 0; s < 2; ++s) {
    mrun[s] = -1e30f;
#pragma unroll
    for (int dt = 0; dt < 2; ++dt) oacc[s][dt] = (f32x4){0.f, 0.f, 0.f, 0.f};
  }
  const float cs = 0.23570226039551584f * 1.44269504088896f;

  {
    *(uint4*)(&sK[0][tid * 8]) = *(const uint4*)(Kh + tid * 8);
    *(uint4*)(&sV[0][tid * 8]) = *(const uint4*)(Vh + tid * 8);
  }
  __syncthreads();
  for (int kt = 0; kt < SEQ / 64; ++kt) {
    int cur = kt & 1;
    uint4 nk, nv;
    if (kt + 1 < SEQ / 64) {
      nk = *(const uint4*)(Kh + (size_t)(kt + 1) * 2048 + tid * 8);
      nv = *(const uint4*)(Vh + (size_t)(kt + 1) * 2048 + tid * 8);
    }
    bf16x8 kf[4], vf[2][2];
#pragma unroll
    for (int t = 0; t < 4; ++t) kf[t] = *(const bf16x8*)(&sK[cur][(16 * t + col) * 32 + 8 * g]);
#pragma unroll
    for (int k2 = 0; k2 < 2; ++k2)
#pragma unroll
      for (int dt = 0; dt < 2; ++dt)
        vf[k2][dt] = *(const bf16x8*)(&sV[cur][(k2 * 32 + 16 * dt + col) * 32 + 8 * g]);
#pragma unroll
    for (int s = 0; s < 2; ++s) {
      f32x4 sc[4];
#pragma unroll
      for (int t = 0; t < 4; ++t)
        sc[t] = __builtin_amdgcn_mfma_f32_16x16x32_bf16(kf[t], qf[s], (f32x4){0.f, 0.f, 0.f, 0.f}, 0, 0, 0);
      float mx = sc[0][0];
#pragma unroll
      for (int t = 0; t < 4; ++t)
#pragma unroll
        for (int r = 0; r < 4; ++r) mx = fmaxf(mx, sc[t][r]);
      mx = fmaxf(mx, __shfl_xor(mx, 16));
      mx = fmaxf(mx, __shfl_xor(mx, 32));
      float mnew = fmaxf(mrun[s], mx);
      float alpha = __builtin_amdgcn_exp2f((mrun[s] - mnew) * cs);
      mrun[s] = mnew;
      float mc = mnew * cs;
#pragma unroll
      for (int dt = 0; dt < 2; ++dt)
#pragma unroll
        for (int r = 0; r < 4; ++r) oacc[s][dt][r] *= alpha;
#pragma unroll
      for (int k2 = 0; k2 < 2; ++k2) {
        bf16x8 pf;
#pragma unroll
        for (int r = 0; r < 4; ++r) {
          float p0 = __builtin_amdgcn_exp2f(sc[2 * k2][r] * cs - mc);
          float p1 = __builtin_amdgcn_exp2f(sc[2 * k2 + 1][r] * cs - mc);
          pf[r] = (short)f2bf(p0);
          pf[4 + r] = (short)f2bf(p1);
        }
#pragma unroll
        for (int dt = 0; dt < 2; ++dt)
          oacc[s][dt] = __builtin_amdgcn_mfma_f32_16x16x32_bf16(vf[k2][dt], pf, oacc[s][dt], 0, 0, 0);
      }
    }
    if (kt + 1 < SEQ / 64) {
      *(uint4*)(&sK[cur ^ 1][tid * 8]) = nk;
      *(uint4*)(&sV[cur ^ 1][tid * 8]) = nv;
    }
    __syncthreads();
  }
#pragma unroll
  for (int s = 0; s < 2; ++s) {
    float l = __shfl(oacc[s][1][2], col);
    float inv = 1.f / l;
    int q = q0 + 16 * s + col;
    u16* dst = O + (size_t)q * DM + h * DH;
    unsigned v0 = pack2(f2bf(oacc[s][0][0] * inv), f2bf(oacc[s][0][1] * inv));
    unsigned v1 = pack2(f2bf(oacc[s][0][2] * inv), f2bf(oacc[s][0][3] * inv));
    *(unsigned*)(dst + 4 * g) = v0;
    *(unsigned*)(dst + 4 * g + 2) = v1;
    if (g == 0) *(unsigned*)(dst + 16) = pack2(f2bf(oacc[s][1][0] * inv), f2bf(oacc[s][1][1] * inv));
  }
}

constexpr int G3_LDA = 72;
__global__ __launch_bounds__(256) void oproj_kernel(const u16* __restrict__ Oin, const u16* __restrict__ wo,
                                                    const u16* __restrict__ x, const u16* __restrict__ gw,
                                                    const u16* __restrict__ BT, u16* __restrict__ out) {
  __shared__ __attribute__((aligned(16))) u16 sA[64 * G3_LDA];
  __shared__ __attribute__((aligned(16))) u16 sB[64 * G3_LDA];
  __shared__ float sG[64];
  const int tid = threadIdx.x;
  const int lane = tid & 63, wv = tid >> 6;
  const int col = lane & 15, g = lane >> 4;
  const int m0 = (blockIdx.x / 9) * 64, n0 = (blockIdx.x % 9) * 64;
  const int wr = wv >> 1, wc = wv & 1;

  {
    int r = tid >> 2, part = tid & 3;
    const u16* xr = x + (size_t)(m0 + r) * DM + part * 144;
    const u16* gp = gw + part * 144;
    float s = 0.f;
    for (int i = 0; i < 144; ++i) s += rbf(bf2f(xr[i]) * bf2f(gp[i]));
    s += __shfl_xor(s, 1);
    s += __shfl_xor(s, 2);
    if (part == 0) {
      float z = rbf(s);
      sG[r] = rbf(1.f / (1.f + expf(-z)));
    }
  }
  f32x4 acc[2][2];
#pragma unroll
  for (int a = 0; a < 2; ++a)
#pragma unroll
    for (int b = 0; b < 2; ++b) acc[a][b] = (f32x4){0.f, 0.f, 0.f, 0.f};
  for (int k0 = 0; k0 < DM; k0 += 64) {
#pragma unroll
    for (int i = 0; i < 2; ++i) {
      int c = tid + 256 * i;
      int r = c >> 3, kc = c & 7;
      *(uint4*)(sA + r * G3_LDA + kc * 8) = *(const uint4*)(Oin + (size_t)(m0 + r) * DM + k0 + kc * 8);
    }
    {
      int kp = tid >> 3, ch = tid & 7;
      const u16* src = wo + (size_t)(k0 + 2 * kp) * DM + n0 + ch * 8;
      uint4 v0 = *(const uint4*)(src);
      uint4 v1 = *(const uint4*)(src + DM);
      const u16* a0 = (const u16*)&v0;
      const u16* a1 = (const u16*)&v1;
#pragma unroll
      for (int i = 0; i < 8; ++i) *(unsigned*)(sB + (ch * 8 + i) * G3_LDA + 2 * kp) = pack2(a0[i], a1[i]);
    }
    __syncthreads();
#pragma unroll
    for (int kk = 0; kk < 2; ++kk) {
      bf16x8 af[2], bfr[2];
#pragma unroll
      for (int i = 0; i < 2; ++i) {
        af[i] = *(const bf16x8*)(sA + (32 * wr + 16 * i + col) * G3_LDA + kk * 32 + 8 * g);
        bfr[i] = *(const bf16x8*)(sB + (32 * wc + 16 * i + col) * G3_LDA + kk * 32 + 8 * g);
      }
#pragma unroll
      for (int a = 0; a < 2; ++a)
#pragma unroll
        for (int b = 0; b < 2; ++b)
          acc[a][b] = __builtin_amdgcn_mfma_f32_16x16x32_bf16(af[a], bfr[b], acc[a][b], 0, 0, 0);
    }
    __syncthreads();
  }
  for (int it = tid; it < 64 * 8; it += 256) {
    int n = it >> 3, c = it & 7;
    *(uint4*)(sA + n * G3_LDA + c * 8) = *(const uint4*)(BT + (size_t)(n0 + n) * SEQ + m0 + c * 8);
  }
  __syncthreads();
#pragma unroll
  for (int a = 0; a < 2; ++a)
#pragma unroll
    for (int b = 0; b < 2; ++b)
#pragma unroll
      for (int r = 0; r < 4; ++r) {
        int row = 32 * wr + 16 * a + 4 * g + r;
        int cc = 32 * wc + 16 * b + col;
        float gv = sG[row];
        float av = rbf(acc[a][b][r]);
        float bv = bf2f(sA[cc * G3_LDA + row]);
        float o = rbf(gv * av) + rbf(rbf(1.f - gv) * bv);
        out[(size_t)(m0 + row) * DM + n0 + cc] = f2bf(o);
      }
}

extern "C" void kernel_launch(void* const* d_in, const int* in_sizes, int n_in, void* d_out, int out_size,
                              void* d_ws, size_t ws_size, hipStream_t stream) {
  const u16* x = (const u16*)d_in[0];
  const u16* wqkv = (const u16*)d_in[1];
  const u16* wo = (const u16*)d_in[2];
  const u16* lar = (const u16*)d_in[4];
  const u16* aim = (const u16*)d_in[5];
  const u16* cre = (const u16*)d_in[6];
  const u16* cim = (const u16*)d_in[7];
  const u16* ldt = (const u16*)d_in[8];
  const u16* dsk = (const u16*)d_in[9];
  const u16* gw = (const u16*)d_in[10];
  char* ws = (char*)d_ws;
  u16* Qp = (u16*)(ws + WS_QP);
  u16* Kp = (u16*)(ws + WS_KP);
  u16* Vt = (u16*)(ws + WS_VT);
  u16* Ob = (u16*)(ws + WS_O);
  u16* BT = (u16*)(ws + WS_BT);

  hipLaunchKernelGGL(k1_kernel, dim3(SSM_BLOCKS + GEMM1_BLOCKS), dim3(256), SMEM1, stream, x, wqkv, lar, aim,
                     cre, cim, ldt, dsk, Qp, Kp, Vt, BT);
  hipLaunchKernelGGL(attn_kernel, dim3(NH * 16), dim3(256), 0, stream, Qp, Kp, Vt, Ob);
  hipLaunchKernelGGL(oproj_kernel, dim3(32 * 9), dim3(256), 0, stream, Ob, wo, x, gw, BT, (u16*)d_out);
}
```

```cpp
#include <hip/hip_runtime.h>
#include <hip/hip_bf16.h>
#include <stdint.h>

typedef __attribute__((ext_vector_type(8))) short bf16x8;
typedef __attribute__((ext_vector_type(4))) short s16x4;
typedef __attribute__((ext_vector_type(4))) float f32x4;
typedef __attribute__((ext_vector_type(2))) float f32x2;
typedef __attribute__((ext_vector_type(2))) __bf16 bf16x2_t;
typedef unsigned short u16;
typedef __attribute__((ext_vector_type(4))) unsigned u32x4;

#define ROLE_QKV 1
#define QX_MAIN 6
#define QX_EPI 1
#define QX_GATE 1
#define KX_E 8
#define KX_W 4
#define KX_OUT 1
#define ROLE_KGEN 1
#define ROLE_TR 1
#define ROLE_ATT 1
#define ROLE_CONV 1
constexpr int SEQ = 2048, DM = 576, NH = 32, DH = 18, NST = 128;

__device__ __forceinline__ float bf2f(u16 v) { return __uint_as_float(((unsigned)v) << 16); }
__device__ __forceinline__ unsigned cvt2(float a, float b) {
  f32x2 v = {a, b};
  bf16x2_t r = __builtin_convertvector(v, bf16x2_t);
  return __builtin_bit_cast(unsigned, r);
}
__device__ __forceinline__ u16 f2bf(float f) { return (u16)(cvt2(f, 0.f) & 0xFFFFu); }
__device__ __forceinline__ float rbf(float f) { return bf2f(f2bf(f)); }
__device__ __forceinline__ unsigned pack2(u16 a, u16 b) { return (unsigned)a | ((unsigned)b << 16); }
__device__ __forceinline__ s16x4 trread(const u16* p) {
  return __builtin_amdgcn_ds_read_tr16_b64_v4i16((__attribute__((address_space(3))) s16x4*)(p));
}

constexpr int CS = 2096;
constexpr size_t WS_QP = 0;
constexpr size_t WS_KP = WS_QP + (size_t)NH * SEQ * 32 * 2;
constexpr size_t WS_VT = WS_KP + (size_t)NH * SEQ * 32 * 2;
constexpr size_t WS_O = WS_VT + (size_t)NH * SEQ * 32 * 2;
constexpr size_t WS_BT = WS_O + (size_t)SEQ * DM * 2;
constexpr size_t WS_XT = WS_BT + (size_t)SEQ * DM * 2;
constexpr size_t WS_RH = WS_XT + (size_t)SEQ * DM * 2;
constexpr size_t WS_RL = WS_RH + (size_t)DM * CS * 2;
constexpr size_t WS_G = WS_RL + (size_t)DM * CS * 2;

constexpr int GEMM1_BLOCKS = (SEQ / 64) * 24;
constexpr int KGEN_BLOCKS = DM;
constexpr int TR_BLOCKS = (SEQ / 64) * (DM / 64);

constexpr int G1_LDA = 104, G1_LDB = 88;
constexpr int SMEM1 = 32768;

__device__ void qkv_block(int bid, const u16* __restrict__ x, const u16* __restrict__ w,
                          const u16* __restrict__ gw, u16* __restrict__ Qp, u16* __restrict__ Kp,
                          u16* __restrict__ Vt, float* __restrict__ G, char* smem) {
  const int tid = threadIdx.x;
  const int lane = tid & 63, wv = tid >> 6;
  const int col = lane & 15, g = lane >> 4;
  const int mt = bid / 24, ntile = bid % 24;
  const int m0 = mt * 64, n0 = ntile * 72;
  u16* sA = (u16*)smem;
  u16* sB = sA + 64 * G1_LDA;

  f32x4 acc[5];
#pragma unroll
  for (int b = 0; b < 5; ++b) acc[b] = (f32x4){0.f, 0.f, 0.f, 0.f};

  u32x4 ra[3], rb[4];
  const u16* gA = x + (size_t)(m0 + (tid / 24)) * DM + (tid % 24) * 8;
  const int q4 = col >> 2, p4 = col & 3;
#define G1LOAD(k0)                                                                                     \
  do {                                                                                                 \
    _Pragma("unroll") for (int i = 0; i < 3; ++i) {                                                    \
      int c = tid + 256 * i;                                                                           \
      ra[i] = *(const u32x4*)(x + (size_t)(m0 + c / 12) * DM + (k0) + (c % 12) * 8);                   \
    }                                                                                                  \
    _Pragma("unroll") for (int i = 0; i < 4; ++i) {                                                    \
      int c = tid + 256 * i;                                                                           \
      if (i < 3 || tid < 96) rb[i] = *(const u32x4*)(w + (size_t)((k0) + c / 9) * (3 * DM) + n0 + (c % 9) * 8); \
    }                                                                                                  \
  } while (0)
  (void)gA;
  G1LOAD(0);
#pragma unroll
  for (int kt = 0; kt < QX_MAIN; ++kt) {
    if (kt) __syncthreads();
#pragma unroll
    for (int i = 0; i < 3; ++i) {
      int c = tid + 256 * i;
      *(u32x4*)(sA + (c / 12) * G1_LDA + (c % 12) * 8) = ra[i];
    }
#pragma unroll
    for (int i = 0; i < 4; ++i) {
      int c = tid + 256 * i;
      if (i < 3 || tid < 96) *(u32x4*)(sB + (c / 9) * G1_LDB + (c % 9) * 8) = rb[i];
    }
    __syncthreads();
    if (kt + 1 < 6) G1LOAD((kt + 1) * 96);
#pragma unroll
    for (int kk = 0; kk < 3; ++kk) {
      bf16x8 af = *(const bf16x8*)(sA + (16 * wv + col) * G1_LDA + kk * 32 + 8 * g);
#pragma unroll
      for (int ni = 0; ni < 5; ++ni) {
        const u16* bp = sB + (32 * kk + 8 * g + q4) * G1_LDB + 16 * ni + 4 * p4;
        s16x4 b0 = trread(bp);
        s16x4 b1 = trread(bp + 4 * G1_LDB);
        bf16x8 bfr = {b0[0], b0[1], b0[2], b0[3], b1[0], b1[1], b1[2], b1[3]};
        acc[ni] = __builtin_amdgcn_mfma_f32_16x16x32_bf16(af, bfr, acc[ni], 0, 0, 0);
      }
    }
  }
  __syncthreads();
  constexpr int LDC = 88;
  u16* sC = (u16*)smem;
#pragma unroll
  for (int ni = 0; ni < 5; ++ni)
#pragma unroll
    for (int r = 0; r < 4; ++r)
      sC[(16 * wv + 4 * g + r) * LDC + 16 * ni + col] = f2bf(acc[ni][r]);
  __syncthreads();
  const int typ = ntile >> 3;
  const int h0 = (ntile & 7) * 4;
  if (!QX_EPI) {} else if (typ < 2) {
    u16* dst = typ == 0 ? Qp : Kp;
#pragma unroll
    for (int it = tid; it < 1024; it += 256) {
      int c = it & 3, hh = (it >> 2) & 3, r = it >> 4;
      const u16* s = sC + r * LDC + hh * 18 + c * 8;
      uint4 v = make_uint4(0, 0, 0, 0);
      if (c < 2) {
        v.x = *(const unsigned*)(s);
        v.y = *(const unsigned*)(s + 2);
        v.z = *(const unsigned*)(s + 4);
        v.w = *(const unsigned*)(s + 6);
      } else if (c == 2) {
        v.x = *(const unsigned*)(s);
      }
      *(uint4*)(dst + ((size_t)(h0 + hh) * SEQ + m0 + r) * 32 + c * 8) = v;
    }
  } else {
#pragma unroll
    for (int it = tid; it < 1024; it += 256) {
      int gg = it & 3, dh = (it >> 2) & 31, kt = (it >> 7) & 1, hh = it >> 8;
      u16 e[8];
#pragma unroll
      for (int j = 0; j < 8; ++j) {
        int key = (j < 4) ? (4 * gg + j) : (16 + 4 * gg + j - 4);
        u16 val;
        if (dh < 18) val = sC[(kt * 32 + key) * LDC + hh * 18 + dh];
        else val = (dh == 18) ? (u16)0x3F80 : (u16)0;
        e[j] = val;
      }
      uint4 v = make_uint4(pack2(e[0], e[1]), pack2(e[2], e[3]), pack2(e[4], e[5]), pack2(e[6], e[7]));
      size_t kt_g = (size_t)(m0 / 32 + kt);
      *(uint4*)(Vt + (((size_t)(h0 + hh) * 64 + kt_g) * 32 + dh) * 32 + gg * 8) = v;
    }
  }
  if (QX_GATE && ntile == 1) {
    int r = tid >> 2, hf = tid & 3;
    const uint4* xr = (const uint4*)(x + (size_t)(m0 + r) * DM + hf * 144);
    const uint4* gp = (const uint4*)(gw + hf * 144);
    float s = 0.f;
#pragma unroll
    for (int i = 0; i < 18; ++i) {
      uint4 a = xr[i], b = gp[i];
      const unsigned av[4] = {a.x, a.y, a.z, a.w}, bv[4] = {b.x, b.y, b.z, b.w};
#pragma unroll
      for (int j = 0; j < 4; ++j) {
        s += rbf(__uint_as_float(av[j] << 16) * __uint_as_float(bv[j] << 16));
        s += rbf(__uint_as_float(av[j] & 0xFFFF0000u) * __uint_as_float(bv[j] & 0xFFFF0000u));
      }
    }
    s += __shfl_xor(s, 1);
    s += __shfl_xor(s, 2);
    if (hf == 0) {
      float z = rbf(s);
      G[m0 + r] = rbf(1.f / (1.f + __expf(-z)));
    }
  }
}

__device__ void kgen_block(int d, const u16* __restrict__ lar, const u16* __restrict__ aim,
                           const u16* __restrict__ cre, const u16* __restrict__ cim,
                           const u16* __restrict__ ldt, u16* __restrict__ Rh, u16* __restrict__ Rl, char* smem) {
  const int tid = threadIdx.x;
  const int lane = tid & 63, wv = tid >> 6;
  const int col = lane & 15, g = lane >> 4;
  float4* prm = (float4*)smem;
  u16* Eh = (u16*)(smem + 2048);
  u16* El = Eh + 4 * 2 * 64 * 8;
  float* Kint = (float*)(smem + 2048 + 16384);

  const float dt = rbf(__expf(bf2f(ldt[d])));
  if (tid < NST) {
    float ar = -rbf(__expf(bf2f(lar[d * NST + tid]))) * dt;
    float ai = bf2f(aim[d * NST + tid]) * dt;
    float cr = bf2f(cre[d * NST + tid]) * dt;
    float ci = bf2f(cim[d * NST + tid]) * dt;
    prm[tid] = make_float4(ar * 1.44269504088896f, ai * 0.15915494309189535f, 2.f * cr, 2.f * ci);
  }
  __syncthreads();
  f32x4 acc[2] = {(f32x4){0.f, 0.f, 0.f, 0.f}, (f32x4){0.f, 0.f, 0.f, 0.f}};
  const float ta = (float)(32 * (16 * wv + col));
  float k2048 = 0.f;
  if (wv == 3) {
#pragma unroll
    for (int i = 0; i < 2; ++i) {
      float4 p = prm[lane + 64 * i];
      float ex = __builtin_amdgcn_exp2f(p.x * 2048.f);
      float ph = p.y * 2048.f;
      k2048 += ex * (p.z * __builtin_amdgcn_cosf(ph) - p.w * __builtin_amdgcn_sinf(ph));
    }
#pragma unroll
    for (int o = 32; o > 0; o >>= 1) k2048 += __shfl_xor(k2048, o);
  }
#pragma unroll
  for (int hf = 0; hf < 2; ++hf) {
  if (hf) __syncthreads();
#pragma unroll 4
  for (int i = 0; i < KX_E; ++i) {
    int e = tid + 256 * i;
    int n = e >> 5, b = e & 31;
    float4 p = prm[64 * hf + n];
    float bf = (float)b;
    float ex = __builtin_amdgcn_exp2f(p.x * bf);
    float ph = p.y * bf;
    float er = ex * __builtin_amdgcn_cosf(ph), ei = ex * __builtin_amdgcn_sinf(ph);
    unsigned hh = cvt2(er, ei);
    float lr = er - __uint_as_float(hh << 16), li = ei - __uint_as_float(hh & 0xFFFF0000u);
    unsigned ll = cvt2(lr, li);
    int off = (((n >> 4) * 2 + (b >> 4)) * 64 + ((n >> 2) & 3) * 16 + (b & 15)) * 8 + 2 * (n & 3);
    *(unsigned*)(Eh + off) = hh;
    *(unsigned*)(El + off) = ll;
  }
  __syncthreads();
#pragma unroll 2
  for (int ks = 0; ks < KX_W; ++ks) {
    bf16x8 ah, al;
#pragma unroll
    for (int i = 0; i < 4; ++i) {
      float4 p = prm[64 * hf + 16 * ks + 4 * g + i];
      float ex = __builtin_amdgcn_exp2f(p.x * ta);
      float ph = p.y * ta;
      float c = __builtin_amdgcn_cosf(ph), s = __builtin_amdgcn_sinf(ph);
      float wr = ex * (p.z * c - p.w * s);
      float wi = -ex * (p.z * s + p.w * c);
      unsigned hh = cvt2(wr, wi);
      float lr = wr - __uint_as_float(hh << 16), li = wi - __uint_as_float(hh & 0xFFFF0000u);
      unsigned ll = cvt2(lr, li);
      ah[2 * i] = (short)(hh & 0xFFFF); ah[2 * i + 1] = (short)(hh >> 16);
      al[2 * i] = (short)(ll & 0xFFFF); al[2 * i + 1] = (short)(ll >> 16);
    }
#pragma unroll
    for (int nt = 0; nt < 2; ++nt) {
      bf16x8 bh = *(const bf16x8*)(Eh + ((ks * 2 + nt) * 64 + lane) * 8);
      bf16x8 bl = *(const bf16x8*)(El + ((ks * 2 + nt) * 64 + lane) * 8);
      acc[nt] = __builtin_amdgcn_mfma_f32_16x16x32_bf16(ah, bh, acc[nt], 0, 0, 0);
      acc[nt] = __builtin_amdgcn_mfma_f32_16x16x32_bf16(ah, bl, acc[nt], 0, 0, 0);
      acc[nt] = __builtin_amdgcn_mfma_f32_16x16x32_bf16(al, bh, acc[nt], 0, 0, 0);
    }
  }
  }
  if (tid == 192) Kint[2048] = k2048;
#pragma unroll
  for (int nt = 0; nt < 2; ++nt)
#pragma unroll
    for (int r = 0; r < 4; ++r) Kint[32 * (16 * wv + 4 * g + r) + 16 * nt + col] = acc[nt][r];
  __syncthreads();
  for (int pb = tid; KX_OUT && pb < CS / 8; pb += 256) {
    unsigned hw[4], lw[4];
#pragma unroll
    for (int i = 0; i < 4; ++i) {
      float v[2];
#pragma unroll
      for (int j = 0; j < 2; ++j) {
        int l = 2047 - (pb * 8 + 2 * i + j);
        v[j] = (l < 0) ? 0.f : Kint[(int)rbf((float)l)];
      }
      unsigned hh = cvt2(v[0], v[1]);
      hw[i] = hh;
      lw[i] = cvt2(v[0] - __uint_as_float(hh << 16), v[1] - __uint_as_float(hh & 0xFFFF0000u));
    }
    *(uint4*)(Rh + (size_t)d * CS + pb * 8) = make_uint4(hw[0], hw[1], hw[2], hw[3]);
    *(uint4*)(Rl + (size_t)d * CS + pb * 8) = make_uint4(lw[0], lw[1], lw[2], lw[3]);
  }
}

__device__ void tr_block(int b, const u16* __restrict__ x, u16* __restrict__ xT, char* smem) {
  u16* s = (u16*)smem;
  const int tid = threadIdx.x;
  const int l0 = (b / 9) * 64, d0 = (b % 9) * 64;
#pragma unroll
  for (int i = 0; i < 2; ++i) {
    int c = tid + 256 * i;
    *(uint4*)(s + (c >> 3) * 72 + (c & 7) * 8) = *(const uint4*)(x + (size_t)(l0 + (c >> 3)) * DM + d0 + (c & 7) * 8);
  }
  __syncthreads();
#pragma unroll
  for (int i = 0; i < 2; ++i) {
    int c = tid + 256 * i;
    int dd = c >> 3, lc = c & 7;
    u16 e[8];
#pragma unroll
    for (int j = 0; j < 8; ++j) e[j] = s[(lc * 8 + j) * 72 + dd];
    *(uint4*)(xT + (size_t)(d0 + dd) * SEQ + l0 + lc * 8) =
        make_uint4(pack2(e[0], e[1]), pack2(e[2], e[3]), pack2(e[4], e[5]), pack2(e[6], e[7]));
  }
}

__global__ __launch_bounds__(256, 4) void k1_kernel(const u16* __restrict__ x, const u16* __restrict__ wqkv,
                                                 const u16* __restrict__ gw, const u16* __restrict__ lar,
                                                 const u16* __restrict__ aim, const u16* __restrict__ cre,
                                                 const u16* __restrict__ cim, const u16* __restrict__ ldt,
                                                 char* __restrict__ ws) {
  extern __shared__ __attribute__((aligned(16))) char smem[];
  int bid = blockIdx.x;
  if (bid < GEMM1_BLOCKS) {
    if (ROLE_QKV) qkv_block(bid, x, wqkv, gw, (u16*)(ws + WS_QP), (u16*)(ws + WS_KP), (u16*)(ws + WS_VT), (float*)(ws + WS_G), smem);
  } else if (bid < GEMM1_BLOCKS + KGEN_BLOCKS) {
    if (ROLE_KGEN) kgen_block(bid - GEMM1_BLOCKS, lar, aim, cre, cim, ldt, (u16*)(ws + WS_RH), (u16*)(ws + WS_RL), smem);
  } else {
    if (ROLE_TR) tr_block(bid - GEMM1_BLOCKS - KGEN_BLOCKS, x, (u16*)(ws + WS_XT), smem);
  }
}

#define NSUB 2
constexpr int ATT_BLOCKS = NH * 32 / NSUB;
constexpr int CONV_BLOCKS = DM;
constexpr int L_U = 0;
constexpr int L_TZ = 5120;
constexpr int SMEM2 = L_TZ + 8 * CS * 2;

__device__ void conv_block(int d, const u16* __restrict__ xT, const u16* __restrict__ Rh,
                           const u16* __restrict__ Rl, const u16* __restrict__ dsk, u16* __restrict__ BT, char* smem) {
  const int tid = threadIdx.x;
  const int lane = tid & 63, wv = tid >> 6;
  u16* Ul = (u16*)(smem + L_U);
  u16* Tz = (u16*)(smem + L_TZ);
  uint4 uv = *(const uint4*)(xT + (size_t)d * SEQ + tid * 8);
  uint4 ka[2], kb[2], ta[2], tb[2];
#pragma unroll
  for (int hl = 0; hl < 2; ++hl) {
    const u16* R = (hl ? Rl : Rh) + (size_t)d * CS;
    ka[hl] = *(const uint4*)(R + tid * 8);
    kb[hl] = *(const uint4*)(R + tid * 8 + 8);
    ta[hl] = make_uint4(0, 0, 0, 0);
    tb[hl] = make_uint4(0, 0, 0, 0);
    if (tid < 6) ta[hl] = *(const uint4*)(R + (256 + tid) * 8);
    if (tid < 5) tb[hl] = *(const uint4*)(R + (256 + tid) * 8 + 8);
  }
  if (tid < 64) *(uint4*)(Ul + tid * 8) = make_uint4(0, 0, 0, 0);
  *(uint4*)(Ul + 512 + tid * 8) = uv;

  f32x4 acc[2][4];
#pragma unroll
  for (int a = 0; a < 2; ++a)
#pragma unroll
    for (int b = 0; b < 4; ++b) acc[a][b] = (f32x4){0.f, 0.f, 0.f, 0.f};
  const int col = lane & 15, g = lane >> 4;
  const int msh = (7 - col) & 7;
#pragma unroll
  for (int hl = 0; hl < 2; ++hl) {
    if (hl) __syncthreads();
#pragma unroll
    for (int part = 0; part < 2; ++part) {
      if (part == 1 && tid >= 6) break;
      uint4 a = part ? ta[hl] : ka[hl];
      uint4 b = part ? tb[hl] : kb[hl];
      int pb = part ? 256 + tid : tid;
      unsigned w[8] = {a.x, a.y, a.z, a.w, b.x, b.y, b.z, b.w};
#pragma unroll
      for (int m = 0; m < 8; ++m) {
        unsigned o[4];
#pragma unroll
        for (int i = 0; i < 4; ++i) {
          int e = m + 2 * i;
          if ((m & 1) == 0) o[i] = w[e >> 1];
          else o[i] = (w[e >> 1] >> 16) | (w[(e >> 1) + 1] << 16);
        }
        *(uint4*)(Tz + m * CS + pb * 8) = make_uint4(o[0], o[1], o[2], o[3]);
      }
    }
    __syncthreads();
    for (int dl = wv; dl < 64; dl += 4) {
      bf16x8 ah[2];
#pragma unroll
      for (int rt = 0; rt < 2; ++rt) {
        int o = 2047 - 32 * dl - (16 * rt + col) + 8 * g - msh;
        ah[rt] = *(const bf16x8*)(Tz + msh * CS + o);
      }
      int nt0 = dl >> 4;
#pragma unroll
      for (int nt = 0; nt < 4; ++nt) {
        if (nt >= nt0) {
          int ch = 16 * nt + col - dl + 16;
          bf16x8 b = *(const bf16x8*)(Ul + ch * 32 + 8 * g);
#pragma unroll
          for (int rt = 0; rt < 2; ++rt)
            acc[rt][nt] = __builtin_amdgcn_mfma_f32_16x16x32_bf16(ah[rt], b, acc[rt][nt], 0, 0, 0);
        }
      }
    }
  }
  __syncthreads();
  float* part = (float*)(smem + L_TZ);
#pragma unroll
  for (int rt = 0; rt < 2; ++rt)
#pragma unroll
    for (int nt = 0; nt < 4; ++nt) {
      int l0 = 32 * (16 * nt + col) + 16 * rt + 4 * g;
      *(f32x4*)(part + wv * 2048 + l0) = acc[rt][nt];
    }
  __syncthreads();
  const float dk = bf2f(dsk[d]);
  {
    const unsigned uw[4] = {uv.x, uv.y, uv.z, uv.w};
    unsigned ow[4];
#pragma unroll
    for (int i = 0; i < 4; ++i) {
      float y[2];
#pragma unroll
      for (int j = 0; j < 2; ++j) {
        int l = tid * 8 + 2 * i + j;
        y[j] = part[l] + part[2048 + l] + part[4096 + l] + part[6144 + l];
      }
      float u0 = __uint_as_float(uw[i] << 16), u1 = __uint_as_float(uw[i] & 0xFFFF0000u);
      float r0 = rbf(y[0]) + rbf(u0 * dk), r1 = rbf(y[1]) + rbf(u1 * dk);
      ow[i] = cvt2(r0, r1);
    }
    *(uint4*)(BT + (size_t)d * SEQ + tid * 8) = make_uint4(ow[0], ow[1], ow[2], ow[3]);
  }
}

__device__ void attn_block(int bid, const u16* __restrict__ Qp, const u16* __restrict__ Kp,
                           const u16* __restrict__ Vt, u16* __restrict__ O, char* smem) {
  u16* sK = (u16*)smem;
  u16* sV = sK + 2 * 64 * 32;
  const int tid = threadIdx.x;
  const int lane = tid & 63, wv = tid >> 6;
  const int col = lane & 15, g = lane >> 4;
  const int h = (bid & 7) + 8 * ((bid >> 3) & 3);
  const int qt = bid >> 5;
  const int q0 = qt * 64 * NSUB + wv * 16 * NSUB;
  const u16* Kh = Kp + (size_t)h * SEQ * 32;
  const u16* Vh = Vt + (size_t)h * SEQ * 32;

  bf16x8 qf[NSUB];
#pragma unroll
  for (int s = 0; s < NSUB; ++s)
    qf[s] = *(const bf16x8*)(Qp + ((size_t)h * SEQ + q0 + 16 * s + col) * 32 + 8 * g);

  f32x4 oacc[NSUB][2];
  float mc[NSUB];
#pragma unroll
  for (int s = 0; s < NSUB; ++s) {
    mc[s] = -1e30f;
#pragma unroll
    for (int dt = 0; dt < 2; ++dt) oacc[s][dt] = (f32x4){0.f, 0.f, 0.f, 0.f};
  }
  const float cs = 0.23570226039551584f * 1.44269504088896f;

  *(uint4*)(&sK[tid * 8]) = *(const uint4*)(Kh + tid * 8);
  *(uint4*)(&sV[tid * 8]) = *(const uint4*)(Vh + tid * 8);
  __syncthreads();
  for (int kt = 0; kt < SEQ / 64; ++kt) {
    const int cur = kt & 1;
    uint4 nk, nv;
    if (kt + 1 < SEQ / 64) {
      nk = *(const uint4*)(Kh + (size_t)(kt + 1) * 2048 + tid * 8);
      nv = *(const uint4*)(Vh + (size_t)(kt + 1) * 2048 + tid * 8);
    }
    const u16* cK = sK + cur * 2048;
    const u16* cV = sV + cur * 2048;
    bf16x8 kf[4], vf[2][2];
#pragma unroll
    for (int t = 0; t < 4; ++t) kf[t] = *(const bf16x8*)(cK + (16 * t + col) * 32 + 8 * g);
#pragma unroll
    for (int k2 = 0; k2 < 2; ++k2)
#pragma unroll
      for (int dt = 0; dt < 2; ++dt) vf[k2][dt] = *(const bf16x8*)(cV + (k2 * 32 + 16 * dt + col) * 32 + 8 * g);
#pragma unroll
    for (int s = 0; s < NSUB; ++s) {
      f32x4 sc[4];
#pragma unroll
      for (int t = 0; t < 4; ++t)
        sc[t] = __builtin_amdgcn_mfma_f32_16x16x32_bf16(kf[t], qf[s], (f32x4){0.f, 0.f, 0.f, 0.f}, 0, 0, 0);
      float mx = sc[0][0];
#pragma unroll
      for (int t = 0; t < 4; ++t)
#pragma unroll
        for (int r = 0; r < 4; ++r) mx = fmaxf(mx, sc[t][r]);
      mx *= cs;
      if (__any(mx > mc[s] + 8.f)) {
        mx = fmaxf(mx, __shfl_xor(mx, 16));
        mx = fmaxf(mx, __shfl_xor(mx, 32));
        float mnew = fmaxf(mc[s], mx);
        float alpha = __builtin_amdgcn_exp2f(mc[s] - mnew);
        mc[s] = mnew;
#pragma unroll
        for (int dt = 0; dt < 2; ++dt)
#pragma unroll
          for (int r = 0; r < 4; ++r) oacc[s][dt][r] *= alpha;
      }
#pragma unroll
      for (int k2 = 0; k2 < 2; ++k2) {
        float p0[4], p1[4];
#pragma unroll
        for (int r = 0; r < 4; ++r) {
          p0[r] = __builtin_amdgcn_exp2f(sc[2 * k2][r] * cs - mc[s]);
          p1[r] = __builtin_amdgcn_exp2f(sc[2 * k2 + 1][r] * cs - mc[s]);
        }
        uint4 pw = make_uint4(cvt2(p0[0], p0[1]), cvt2(p0[2], p0[3]), cvt2(p1[0], p1[1]), cvt2(p1[2], p1[3]));
        bf16x8 pf = __builtin_bit_cast(bf16x8, pw);
#pragma unroll
        for (int dt = 0; dt < 2; ++dt)
          oacc[s][dt] = __builtin_amdgcn_mfma_f32_16x16x32_bf16(vf[k2][dt], pf, oacc[s][dt], 0, 0, 0);
      }
    }
    if (kt + 1 < SEQ / 64) {
      *(uint4*)(&sK[(cur ^ 1) * 2048 + tid * 8]) = nk;
      *(uint4*)(&sV[(cur ^ 1) * 2048 + tid * 8]) = nv;
    }
    __syncthreads();
  }
#pragma unroll
  for (int s = 0; s < NSUB; ++s) {
    float l = __shfl(oacc[s][1][2], col);
    float inv = 1.f / l;
    int q = q0 + 16 * s + col;
    u16* dst = O + (size_t)q * DM + h * DH;
    *(unsigned*)(dst + 4 * g) = cvt2(oacc[s][0][0] * inv, oacc[s][0][1] * inv);
    *(unsigned*)(dst + 4 * g + 2) = cvt2(oacc[s][0][2] * inv, oacc[s][0][3] * inv);
    if (g == 0) *(unsigned*)(dst + 16) = cvt2(oacc[s][1][0] * inv, oacc[s][1][1] * inv);
  }
}

__global__ __launch_bounds__(256, 2) void k2_kernel(const u16* __restrict__ dsk, char* __restrict__ ws) {
  extern __shared__ __attribute__((aligned(16))) char smem[];
  int bid = blockIdx.x;
  if (bid < ATT_BLOCKS) {
    if (ROLE_ATT) attn_block(bid, (const u16*)(ws + WS_QP), (const u16*)(ws + WS_KP), (const u16*)(ws + WS_VT), (u16*)(ws + WS_O), smem);
  } else {
    if (ROLE_CONV) conv_block(bid - ATT_BLOCKS, (const u16*)(ws + WS_XT), (const u16*)(ws + WS_RH), (const u16*)(ws + WS_RL), dsk,
               (u16*)(ws + WS_BT), smem);
  }
}

constexpr int G3_LDA = 200, G3_LDB = 72;
__global__ __launch_bounds__(256, 2) void oproj_kernel(const u16* __restrict__ wo, const char* __restrict__ ws,
                                                       u16* __restrict__ out) {
  __shared__ __attribute__((aligned(16))) u16 sA[64 * G3_LDA];
  __shared__ __attribute__((aligned(16))) u16 sB[192 * G3_LDB];
  const u16* Oin = (const u16*)(ws + WS_O);
  const u16* BT = (const u16*)(ws + WS_BT);
  const float* G = (const float*)(ws + WS_G);
  const int tid = threadIdx.x;
  const int lane = tid & 63, wv = tid >> 6;
  const int col = lane & 15, g = lane >> 4;
  const int m0 = (blockIdx.x / 9) * 64, n0 = (blockIdx.x % 9) * 64;
  const int wr = wv >> 1, wc = wv & 1;
  const int q4 = col >> 2, p4 = col & 3;

  f32x4 acc[2][2];
#pragma unroll
  for (int a = 0; a < 2; ++a)
#pragma unroll
    for (int b = 0; b < 2; ++b) acc[a][b] = (f32x4){0.f, 0.f, 0.f, 0.f};
  u32x4 ra[6], rb[6], rbt[2];
#define G3LOAD(k0)                                                                        \
  do {                                                                                    \
    _Pragma("unroll") for (int i = 0; i < 6; ++i) {                                       \
      int c = tid + 256 * i;                                                              \
      ra[i] = *(const u32x4*)(Oin + (size_t)(m0 + c / 24) * DM + (k0) + (c % 24) * 8);    \
      rb[i] = *(const u32x4*)(wo + (size_t)((k0) + (c >> 3)) * DM + n0 + (c & 7) * 8);    \
    }                                                                                     \
  } while (0)
  G3LOAD(0);
#pragma unroll
  for (int i = 0; i < 2; ++i) {
    int it = tid + 256 * i;
    rbt[i] = *(const u32x4*)(BT + (size_t)(n0 + (it >> 3)) * SEQ + m0 + (it & 7) * 8);
  }
  float gv[2][4];
#pragma unroll
  for (int a = 0; a < 2; ++a)
#pragma unroll
    for (int r = 0; r < 4; ++r) gv[a][r] = G[m0 + 32 * wr + 16 * a + 4 * g + r];
#pragma unroll
  for (int kt = 0; kt < 3; ++kt) {
    if (kt) __syncthreads();
#pragma unroll
    for (int i = 0; i < 6; ++i) {
      int c = tid + 256 * i;
      *(u32x4*)(sA + (c / 24) * G3_LDA + (c % 24) * 8) = ra[i];
      *(u32x4*)(sB + (c >> 3) * G3_LDB + (c & 7) * 8) = rb[i];
    }
    __syncthreads();
    if (kt + 1 < 3) G3LOAD((kt + 1) * 192);
#pragma unroll
    for (int kk = 0; kk < 6; ++kk) {
      bf16x8 af[2], bfr[2];
#pragma unroll
      for (int i = 0; i < 2; ++i) {
        af[i] = *(const bf16x8*)(sA + (32 * wr + 16 * i + col) * G3_LDA + kk * 32 + 8 * g);
        const u16* bp = sB + (32 * kk + 8 * g + q4) * G3_LDB + 32 * wc + 16 * i + 4 * p4;
        s16x4 b0 = trread(bp);
        s16x4 b1 = trread(bp + 4 * G3_LDB);
        bfr[i] = (bf16x8){b0[0], b0[1], b0[2], b0[3], b1[0], b1[1], b1[2], b1[3]};
      }
#pragma unroll
      for (int a = 0; a < 2; ++a)
#pragma unroll
        for (int b = 0; b < 2; ++b)
          acc[a][b] = __builtin_amdgcn_mfma_f32_16x16x32_bf16(af[a], bfr[b], acc[a][b], 0, 0, 0);
    }
  }
  __syncthreads();
#pragma unroll
  for (int i = 0; i < 2; ++i) {
    int it = tid + 256 * i;
    *(u32x4*)(sB + (it >> 3) * G3_LDB + (it & 7) * 8) = rbt[i];
  }
  __syncthreads();
#pragma unroll
  for (int a = 0; a < 2; ++a)
#pragma unroll
    for (int b = 0; b < 2; ++b)
#pragma unroll
      for (int r = 0; r < 4; ++r) {
        int row = 32 * wr + 16 * a + 4 * g + r;
        int cc = 32 * wc + 16 * b + col;
        float av = rbf(acc[a][b][r]);
        float bv = bf2f(sB[cc * G3_LDB + row]);
        float o = rbf(gv[a][r] * av) + rbf(rbf(1.f - gv[a][r]) * bv);
        sA[row * G3_LDB + cc] = f2bf(o);
      }
  __syncthreads();
#pragma unroll
  for (int i = 0; i < 2; ++i) {
    int c = tid + 256 * i;
    int r = c >> 3, kc = c & 7;
    *(uint4*)(out + (size_t)(m0 + r) * DM + n0 + kc * 8) = *(const uint4*)(sA + r * G3_LDB + kc * 8);
  }
}

extern "C" void kernel_launch(void* const* d_in, const int* in_sizes, int n_in, void* d_out, int out_size,
                              void* d_ws, size_t ws_size, hipStream_t stream) {
  const u16* x = (const u16*)d_in[0];
  const u16* wqkv = (const u16*)d_in[1];
  const u16* wo = (const u16*)d_in[2];
  const u16* lar = (const u16*)d_in[4];
  const u16* aim = (const u16*)d_in[5];
  const u16* cre = (const u16*)d_in[6];
  const u16* cim = (const u16*)d_in[7];
  const u16* ldt = (const u16*)d_in[8];
  const u16* dsk = (const u16*)d_in[9];
  const u16* gw = (const u16*)d_in[10];
  char* ws = (char*)d_ws;

  hipLaunchKernelGGL(k1_kernel, dim3(GEMM1_BLOCKS + KGEN_BLOCKS + TR_BLOCKS), dim3(256), SMEM1, stream, x, wqkv, gw,
                     lar, aim, cre, cim, ldt, ws);
  hipLaunchKernelGGL(k2_kernel, dim3(ATT_BLOCKS + CONV_BLOCKS), dim3(256), SMEM2, stream, dsk, ws);
  hipLaunchKernelGGL(oproj_kernel, dim3(32 * 9), dim3(256), 0, stream, wo, ws, (u16*)d_out);
}
```
